```python
import torch

def _softmax(x, axis=-1):
    return torch.softmax(x, dim=axis)
def _gelu(x):
    return torch.nn.functional.gelu(x, approximate="tanh")
def _silu(x):
    return torch.nn.functional.silu(x)
def _sigmoid(x):
    return torch.sigmoid(x)
def _layernorm(x, axis=-1, eps=1e-5):
    m = x.mean(dim=axis, keepdim=True)
    v = x.var(dim=axis, keepdim=True, unbiased=False)
    return (x - m) / torch.sqrt(v + eps)
def _scatter_add(tgt, idx, src, axis=0):
    return tgt.clone().index_add_(axis, idx, src)
def _segment_sum(src, seg, n):
    out = torch.zeros((n,) + src.shape[1:], dtype=src.dtype)
    return out.index_add_(0, seg, src)
def _pad(x, pw):
    flat = tuple(v for pair in reversed(pw) for v in pair)
    return torch.nn.functional.pad(x, flat)

kernel_name = 'gated_mix__linear_kernel_hedgehog__softmax_full_mha__prompt'

def setup_inputs():

    _w = (lambda a: (lambda t: t.to(torch.bfloat16) if t.is_floating_point() else t)(torch.as_tensor(a)))
    def _A_setup():
        B, S, D, H = 1, 2048, 512, 32
        Dh = D // H
        return {
            "x": _w((lambda *s: torch.randn(*s, dtype=torch.bfloat16) * 0.05)(B, S, D)),
            "wqkv": _w((lambda *s: torch.randn(*s, dtype=torch.bfloat16) * 3.2)(D, 3 * D)),
            "w_phi": _w((lambda *s: torch.randn(*s, dtype=torch.bfloat16) * 3.2)(H, Dh, Dh) * (Dh ** -0.5)),
            "wo": _w((lambda *s: torch.randn(*s, dtype=torch.bfloat16) * 0.05)(D, D)),
        }
    _A_in = _A_setup()

    def _B_setup():
        B, S, D = 1, 2048, 512
        return {
            "x": _w((lambda *s: torch.randn(*s, dtype=torch.bfloat16) * 0.05)(B, S, D)),
            "wqkv": _w((lambda *s: torch.randn(*s, dtype=torch.bfloat16) * 1.6)(D, 3 * D)),
            "wo": _w((lambda *s: torch.randn(*s, dtype=torch.bfloat16) * 0.05)(D, D)),
        }
    _B_in = _B_setup()

    return {
        "A_x": _A_in["x"],
        "A_wqkv": _A_in["wqkv"],
        "A_w_phi": _A_in["w_phi"],
        "A_wo": _A_in["wo"],
        "B_x": _B_in["x"],
        "B_wqkv": _B_in["wqkv"],
        "B_wo": _B_in["wo"],
        "gate_w": _w((lambda *s: torch.randn(*s, dtype=torch.bfloat16) * 0.05)(512) * 16.0),
    }

def reference(A_x, A_wqkv, A_w_phi, A_wo, B_x, B_wqkv, B_wo, gate_w):
    def _A_ref(x, wqkv, w_phi, wo):
        B, S, D = x.shape
        H = 32; Dh = D // H
        qkv = (lambda x, *d: x.permute(*d))((x @ wqkv).reshape(B, S, 3, H, Dh), 2, 0, 3, 1, 4)
        q, k, v = qkv[0], qkv[1], qkv[2]
        pq = torch.einsum("bhsd,hdf->bhsf", q, w_phi)
        pk = torch.einsum("bhsd,hdf->bhsf", k, w_phi)
        phi_q = torch.concatenate([_softmax(pq, axis=-1), _softmax(-pq, axis=-1)], axis=-1)
        phi_k = torch.concatenate([_softmax(pk, axis=-1), _softmax(-pk, axis=-1)], axis=-1)
        kv = torch.einsum("bhsf,bhsd->bhfd", phi_k, v)
        z  = torch.einsum("bhsf->bhf", phi_k)
        num = torch.einsum("bhqf,bhfd->bhqd", phi_q, kv)
        den = torch.einsum("bhqf,bhf->bhq", phi_q, z)[..., None] + 1e-6
        o = num / den
        return (lambda x, *d: x.permute(*d))(o, 0, 2, 1, 3).reshape(B, S, D) @ wo

    def _B_ref(x, wqkv, wo):
        B, S, D = x.shape
        H = 32; Dh = D // H
        qkv = (lambda x, *d: x.permute(*d))((x @ wqkv).reshape(B, S, 3, H, Dh), 2, 0, 3, 1, 4)
        q, k, v = qkv[0], qkv[1], qkv[2]
        scale = Dh ** -0.5
        scores = torch.einsum("bhqd,bhkd->bhqk", q, k) * scale
        p = _softmax(scores, axis=-1)
        o = torch.einsum("bhqk,bhkd->bhqd", p, v)
        return (lambda x, *d: x.permute(*d))(o, 0, 2, 1, 3).reshape(B, S, D) @ wo

    x = A_x
    g = _sigmoid((x * gate_w).sum(-1, keepdims=True))
    a = _A_ref(x, A_wqkv, A_w_phi, A_wo)
    b = _B_ref(x, B_wqkv, B_wo)
    return g * a + (1 - g) * b
```

```cpp
#include <hip/hip_runtime.h>
#include <hip/hip_bf16.h>
#include <stdint.h>

typedef __attribute__((ext_vector_type(8))) short bf16x8;
typedef __attribute__((ext_vector_type(4))) short s16x4;
typedef __attribute__((ext_vector_type(16))) float f32x16;
typedef __attribute__((ext_vector_type(4))) float f32x4;
typedef __attribute__((ext_vector_type(2))) float f32x2;
typedef __attribute__((ext_vector_type(4))) unsigned u32x4;
typedef __attribute__((ext_vector_type(2))) unsigned u32x2;
typedef __attribute__((ext_vector_type(2))) __bf16 bf16x2_t;
typedef unsigned short u16;
#define LAS __attribute__((address_space(3)))
typedef LAS char* lds_ptr;
#define DI __device__ __forceinline__
#define MFMA(a, b, c) __builtin_amdgcn_mfma_f32_32x32x16_bf16((a), (b), (c), 0, 0, 0)

constexpr int SEQ = 2048, DM = 512, NH = 32;
constexpr float LOG2E = 1.4426950408889634f;
constexpr float QSCALE = 0.25f * LOG2E;

constexpr size_t WS_Q = 0;
constexpr size_t WS_K = WS_Q + (2u << 20);
constexpr size_t WS_V = WS_K + (2u << 20);
constexpr size_t WS_PHIQ = WS_V + (2u << 20);
constexpr size_t WS_KVP = WS_PHIQ + (4u << 20);
constexpr size_t WS_G = WS_KVP + (2u << 20);
constexpr size_t WS_O = WS_G + (64u << 10);
constexpr size_t WS_WO = WS_O + (4u << 20);

DI unsigned cvtpk(float a, float b) {
    f32x2 v = {a, b};
    bf16x2_t r = __builtin_convertvector(v, bf16x2_t);
    return __builtin_bit_cast(unsigned, r);
}
DI float bf2f(u16 v) { return __uint_as_float((unsigned)v << 16); }
DI bf16x8 pack8(const f32x16& x, int s, float sc) {
    u32x4 p;
    p[0] = cvtpk(x[8 * s + 0] * sc, x[8 * s + 1] * sc);
    p[1] = cvtpk(x[8 * s + 2] * sc, x[8 * s + 3] * sc);
    p[2] = cvtpk(x[8 * s + 4] * sc, x[8 * s + 5] * sc);
    p[3] = cvtpk(x[8 * s + 6] * sc, x[8 * s + 7] * sc);
    return __builtin_bit_cast(bf16x8, p);
}
DI bf16x8 pack8n(const f32x16& x, int s) {
    u32x4 p;
    p[0] = cvtpk(x[8 * s + 0], x[8 * s + 1]);
    p[1] = cvtpk(x[8 * s + 2], x[8 * s + 3]);
    p[2] = cvtpk(x[8 * s + 4], x[8 * s + 5]);
    p[3] = cvtpk(x[8 * s + 6], x[8 * s + 7]);
    return __builtin_bit_cast(bf16x8, p);
}
DI bf16x8 lds_rd128(lds_ptr p) { return *(const LAS bf16x8*)p; }
typedef short v4i16_t __attribute__((ext_vector_type(4)));
DI s16x4 lds_tr(lds_ptr p) { return __builtin_bit_cast(s16x4, __builtin_amdgcn_ds_read_tr16_b64_v4i16((LAS v4i16_t*)p)); }
DI bf16x8 cat8(s16x4 lo, s16x4 hi) { return __builtin_shufflevector(lo, hi, 0, 1, 2, 3, 4, 5, 6, 7); }
DI float xhalf_max(float m) {
    auto rr = __builtin_amdgcn_permlane32_swap(__float_as_uint(m), __float_as_uint(m), false, false);
    return __builtin_fmaxf(__uint_as_float(rr[0]), __uint_as_float(rr[1]));
}
DI float xhalf_sum(float m) {
    auto rr = __builtin_amdgcn_permlane32_swap(__float_as_uint(m), __float_as_uint(m), false, false);
    return __uint_as_float(rr[0]) + __uint_as_float(rr[1]);
}
DI void glds16(const void* base, unsigned off, unsigned lds_base) {
    unsigned sv;
    asm volatile("s_mov_b32 %0, m0\n\ts_mov_b32 m0, %3\n\ts_nop 0\n\tglobal_load_lds_dwordx4 %1, %2\n\ts_mov_b32 m0, %0"
                 : "=&s"(sv) : "v"(off), "s"(base), "s"(lds_base) : "memory");
}
#define WAIT_BAR(N) asm volatile("s_waitcnt vmcnt(" #N ") lgkmcnt(0)\n\ts_barrier" ::: "memory")

constexpr int K1_SLOT = 40960, K1_WOFF = 16384;

DI void feat_softmax(f32x16& p) {
#pragma unroll
    for (int s = 0; s < 2; ++s) {
        float m = __builtin_fmaxf(__builtin_fmaxf(p[8 * s], p[8 * s + 1]), __builtin_fmaxf(p[8 * s + 2], p[8 * s + 3]));
        m = __builtin_fmaxf(m, __builtin_fmaxf(__builtin_fmaxf(p[8 * s + 4], p[8 * s + 5]), __builtin_fmaxf(p[8 * s + 6], p[8 * s + 7])));
        m = xhalf_max(m);
        const float nm = -m * LOG2E;
        float sum = 0.f;
#pragma unroll
        for (int j = 0; j < 8; ++j) { p[8 * s + j] = __builtin_amdgcn_exp2f(__builtin_fmaf(p[8 * s + j], LOG2E, nm)); sum += p[8 * s + j]; }
        sum = xhalf_sum(sum);
        const float r = __builtin_amdgcn_rcpf(sum);
#pragma unroll
        for (int j = 0; j < 8; ++j) p[8 * s + j] *= r;
    }
}

__global__ __launch_bounds__(512, 2) void k1_qkv(const u16* __restrict__ x, const u16* __restrict__ wa, const u16* __restrict__ wb,
                                                  const u16* __restrict__ wphi, const u16* __restrict__ gw, char* __restrict__ ws) {
    extern __shared__ __attribute__((aligned(16))) char smem[];
    const lds_ptr lds = (lds_ptr)smem;
    const unsigned lds0 = (unsigned)(uintptr_t)smem;
    const int tid = threadIdx.x, lane = tid & 63, r32 = lane & 31, hi = lane >> 5;
    const int w = __builtin_amdgcn_readfirstlane(tid >> 6);
    const int b = blockIdx.x, xcd = b & 7, bi = b >> 3;
    const int tb = (xcd & 1) * 8 + (bi & 7);
    const int hp = (xcd >> 1) * 4 + (bi >> 3);
    const int tok0 = tb * 128;

    unsigned xoff[2], woff[3];
    const u16* wsrc[3];
#pragma unroll
    for (int i = 0; i < 2; ++i) {
        const int row = 16 * w + 8 * i + (lane >> 3);
        const int c = (lane & 7) ^ ((row >> 1) & 7);
        xoff[i] = (unsigned)((tok0 + row) * DM + c * 8) * 2u;
    }
#pragma unroll
    for (int i = 0; i < 3; ++i) {
        const int idx = 3 * w + i, seg = idx >> 2, rb = idx & 3;
        const int br = seg / 3, part = seg - 3 * br;
        wsrc[i] = br ? wa : wb;
        woff[i] = (unsigned)((16 * rb + (lane >> 2)) * 1536 + part * 512 + hp * 32 + (lane & 3) * 8) * 2u;
    }
    auto issue = [&](int c) {
        const unsigned slot = lds0 + (unsigned)(c & 3) * K1_SLOT;
#pragma unroll
        for (int i = 0; i < 2; ++i) glds16(x, xoff[i] + (unsigned)c * 128u, slot + (unsigned)(16 * w + 8 * i) * 128u);
#pragma unroll
        for (int i = 0; i < 3; ++i) glds16(wsrc[i], woff[i] + (unsigned)c * (64u * 1536u * 2u), slot + K1_WOFF + (unsigned)(3 * w + i) * 1024u);
    };
    issue(0); issue(1); issue(2); issue(3);

    const int sub = w & 3, br = w >> 2;
    const int xrow = 32 * sub + r32;
    const int xsw = (xrow >> 1) & 7;
    const int g16 = lane >> 4, q4 = (lane & 15) >> 2, p4 = lane & 3;
    const unsigned wrd = K1_WOFF + br * 3 * 4096 + (8 * hi + q4) * 64 + (g16 & 1) * 32 + p4 * 8;
    f32x16 aq = {}, ak = {}, av = {};
#pragma unroll
    for (int c = 0; c < 8; ++c) {
        if (c == 0) WAIT_BAR(15);
        else if (c <= 5) WAIT_BAR(10);
        else if (c == 6) WAIT_BAR(5);
        else WAIT_BAR(0);
        if (c >= 1 && c + 3 < 8) issue(c + 3);
        const lds_ptr sl = lds + (c & 3) * K1_SLOT;
#pragma unroll
        for (int ks = 0; ks < 4; ++ks) {
            const bf16x8 xf = lds_rd128(sl + xrow * 128 + (((2 * ks + hi) ^ xsw) * 16));
            const lds_ptr wp = sl + wrd + ks * 1024;
            const bf16x8 wq = cat8(lds_tr(wp), lds_tr(wp + 256));
            const bf16x8 wk = cat8(lds_tr(wp + 4096), lds_tr(wp + 4096 + 256));
            const bf16x8 wv = cat8(lds_tr(wp + 8192), lds_tr(wp + 8192 + 256));
            aq = MFMA(wq, xf, aq);
            ak = MFMA(wk, xf, ak);
            av = MFMA(xf, wv, av);
        }
    }
    const int tile = tb * 4 + sub;
    if (br == 0) {
#pragma unroll
        for (int e = 0; e < 2; ++e) {
            const int head = 2 * hp + e;
            *(bf16x8*)(ws + WS_Q + ((size_t)(head * 64 + tile) * 64 + lane) * 16) = pack8(aq, e, QSCALE);
            *(bf16x8*)(ws + WS_K + ((size_t)(head * 64 + tile) * 64 + lane) * 16) = pack8n(ak, e);
        }
        const int head = 2 * hp + (r32 >> 4), d = r32 & 15;
        const int row = (d & 3) + 8 * ((d >> 2) & 1) + 4 * (d >> 3);
#pragma unroll
        for (int s = 0; s < 2; ++s)
            *(bf16x8*)(ws + WS_V + (size_t)(head * 64 + tile) * 1024 + s * 512 + hi * 256 + row * 16) = pack8n(av, s);
#pragma unroll
        for (int i = 0; i < 2; ++i) {
            const int tok = tok0 + hp * 8 + sub * 2 + i;
            const bf16x8 xv = *(const bf16x8*)(x + (size_t)tok * DM + lane * 8);
            const bf16x8 gv = *(const bf16x8*)(gw + lane * 8);
            float acc = 0.f;
#pragma unroll
            for (int j = 0; j < 8; ++j) acc = __builtin_fmaf(bf2f((u16)xv[j]), bf2f((u16)gv[j]), acc);
#pragma unroll
            for (int m = 32; m >= 1; m >>= 1) acc += __shfl_xor(acc, m);
            if (lane == 0) ((float*)(ws + WS_G))[tok] = 1.f / (1.f + __expf(-acc));
        }
    }
    __syncthreads();
    if (br == 1) {
        const lds_ptr timg = lds + sub * 2048;
        LAS float* red = (LAS float*)(lds + 8192);
        const bf16x8 vf0 = pack8n(av, 0), vf1 = pack8n(av, 1);
        const bf16x8 ones = {0x3F80, 0x3F80, 0x3F80, 0x3F80, 0x3F80, 0x3F80, 0x3F80, 0x3F80};
#pragma unroll
        for (int e = 0; e < 2; ++e) {
            const int head = 2 * hp + e;
            bf16x8 wf;
#pragma unroll
            for (int j = 0; j < 8; ++j) {
                const int d = 8 * (j >> 2) + 4 * hi + (j & 3);
                const u16 v = wphi[(head * 16 + d) * 16 + (r32 & 15)];
                wf[j] = (short)(v ^ ((r32 & 16) ? 0x8000 : 0));
            }
            const f32x16 zero = {};
            f32x16 pq = MFMA(wf, pack8n(aq, e), zero);
            f32x16 pk = MFMA(wf, pack8n(ak, e), zero);
            feat_softmax(pq);
            feat_softmax(pk);
#pragma unroll
            for (int s = 0; s < 2; ++s)
                *(bf16x8*)(ws + WS_PHIQ + ((size_t)((head * 64 + tile) * 2 + s) * 64 + lane) * 16) = pack8n(pq, s);
#pragma unroll
            for (int g = 0; g < 4; ++g) {
                u32x2 v; v[0] = cvtpk(pk[4 * g], pk[4 * g + 1]); v[1] = cvtpk(pk[4 * g + 2], pk[4 * g + 3]);
                *(LAS u32x2*)(timg + r32 * 64 + (8 * g + 4 * hi) * 2) = v;
            }
            asm volatile("s_waitcnt lgkmcnt(0)" ::: "memory");
            const bool mine = (r32 >> 4) == e;
            f32x16 kv = {};
#pragma unroll
            for (int s = 0; s < 2; ++s) {
                const lds_ptr tp = timg + (16 * s + 4 * hi + q4) * 64 + (g16 & 1) * 32 + p4 * 8;
                const bf16x8 af = cat8(lds_tr(tp), lds_tr(tp + 8 * 64));
                const bf16x8 vf = s ? vf1 : vf0;
                bf16x8 bfr;
#pragma unroll
                for (int j = 0; j < 8; ++j) bfr[j] = mine ? vf[j] : ones[j];
                kv = MFMA(af, bfr, kv);
            }
            asm volatile("s_waitcnt lgkmcnt(0)" ::: "memory");
#pragma unroll
            for (int r = 0; r < 16; ++r) red[((sub * 2 + e) * 16 + r) * 64 + lane] = kv[r];
        }
    }
    __syncthreads();
    {
        const LAS float* red = (const LAS float*)(lds + 8192);
#pragma unroll
        for (int i = 0; i < 4; ++i) {
            const int v = tid + 512 * i, e = v >> 10, u = v & 1023;
            const int j = u & 7, l2 = (u >> 3) & 63, s = u >> 9;
            const int row = l2 & 31, h2 = l2 >> 5;
            const int d = (row & 3) + 4 * ((row >> 3) & 1) + 8 * ((row >> 2) & 1);
            const int col = row < 16 ? e * 16 + d : (1 - e) * 16;
            const int r = 8 * s + j;
            float acc = 0.f;
#pragma unroll
            for (int sb = 0; sb < 4; ++sb) acc += red[((sb * 2 + e) * 16 + r) * 64 + h2 * 32 + col];
            ((float*)(ws + WS_KVP))[(size_t)((2 * hp + e) * 16 + tb) * 1024 + u] = acc;
        }
    }
}

constexpr int K2_K = 0, K2_V = 65536, K2_ONES = 131072, K2_Q = K2_ONES + 4096, K2_KV = K2_Q + 8192, K2_LDS = K2_KV + 2048;

__global__ __launch_bounds__(512, 2) void k2_attn(const u16* __restrict__ woa, const u16* __restrict__ wob, char* __restrict__ ws) {
    extern __shared__ __attribute__((aligned(16))) char smem[];
    const lds_ptr lds = (lds_ptr)smem;
    const unsigned lds0 = (unsigned)(uintptr_t)smem;
    const int tid = threadIdx.x, lane = tid & 63, r32 = lane & 31, hi = lane >> 5;
    const int w = __builtin_amdgcn_readfirstlane(tid >> 6);
    const int b = blockIdx.x, xcd = b & 7, bi = b >> 3;
    const int head = xcd * 4 + (bi >> 3), qb = bi & 7;
    const int qtile = qb * 8 + w;

    glds16(ws + WS_Q, (unsigned)((head * 64 + qtile) * 1024 + lane * 16), lds0 + K2_Q + w * 1024);
    {
        const unsigned o = (unsigned)((head * 64 + w) * 1024 + lane * 16);
#pragma unroll
        for (int c = 0; c < 8; ++c) {
            glds16(ws + WS_K, o + c * 8192u, lds0 + K2_K + (unsigned)(8 * c + w) * 1024u);
            glds16(ws + WS_V, o + c * 8192u, lds0 + K2_V + (unsigned)(8 * c + w) * 1024u);
        }
    }
    *(LAS u32x2*)(lds + K2_ONES + tid * 8) = (u32x2){0x3F803F80u, 0x3F803F80u};

    WAIT_BAR(14);
    const bf16x8 qf = lds_rd128(lds + K2_Q + w * 1024 + lane * 16);
    lds_ptr kp = lds + K2_K + lane * 16;
    lds_ptr vp = lds + (r32 < 16 ? K2_V + hi * 256 + r32 * 16 : K2_ONES + hi * 256 + (r32 - 16) * 16);
    const int vinc = r32 < 16 ? 4096 : 0;
    const f32x16 zero = {};
    f32x16 negm;
    float m0;
    {
        const f32x16 s0 = MFMA(lds_rd128(kp), qf, zero);
        float m = s0[0];
#pragma unroll
        for (int r = 1; r < 16; ++r) m = __builtin_fmaxf(m, s0[r]);
        m0 = xhalf_max(m);
#pragma unroll
        for (int r = 0; r < 16; ++r) negm[r] = -m0;
    }
    f32x16 o = {};
    f32x16 sc = MFMA(lds_rd128(kp), qf, negm);
    auto step4 = [&]() {
#pragma unroll
        for (int u = 0; u < 4; ++u) {
            const bf16x8 kn = lds_rd128(kp + (u + 1) * 1024);
            const bf16x8 v0 = lds_rd128(vp + u * 1024);
            const bf16x8 v1 = lds_rd128(vp + u * 1024 + 512);
            const f32x16 sn = MFMA(kn, qf, negm);
#pragma unroll
            for (int r = 0; r < 16; ++r) sc[r] = __builtin_amdgcn_exp2f(sc[r]);
            o = MFMA(v0, pack8n(sc, 0), o);
            o = MFMA(v1, pack8n(sc, 1), o);
            sc = sn;
        }
        kp += 4096; vp += vinc;
    };
    step4();
    WAIT_BAR(0);
#pragma unroll 1
    for (int it = 1; it < 8; ++it) step4();
    float part[2][16];
#pragma unroll
    for (int i = 0; i < 2; ++i)
#pragma unroll
        for (int p = 0; p < 16; ++p) part[i][p] = ((const float*)(ws + WS_KVP))[(size_t)(head * 16 + p) * 1024 + tid + 512 * i];
    const bf16x8 phq0 = *(const bf16x8*)(ws + WS_PHIQ + ((size_t)((head * 64 + qtile) * 2 + 0) * 64 + lane) * 16);
    const bf16x8 phq1 = *(const bf16x8*)(ws + WS_PHIQ + ((size_t)((head * 64 + qtile) * 2 + 1) * 64 + lane) * 16);
    const float gate = ((const float*)(ws + WS_G))[qtile * 32 + r32];
    asm volatile("" ::: "memory");
#pragma unroll 1
    for (int it = 8; it < 16; ++it) step4();
    {
        float amax = 0.f;
#pragma unroll
        for (int r = 0; r < 8; ++r) amax = __builtin_fmaxf(amax, __builtin_fabsf(o[r]));
        const bool ok = (o[8] < 1.0e18f) && (amax < 1.0e37f) && (o[0] == o[0]) && (o[1] == o[1]) && (o[2] == o[2]) && (o[3] == o[3]) &&
                        (o[4] == o[4]) && (o[5] == o[5]) && (o[6] == o[6]) && (o[7] == o[7]) && (o[8] >= 0.5f);
        if (__builtin_expect(!__all(ok), 0)) {
            kp = lds + K2_K + lane * 16;
            vp = lds + (r32 < 16 ? K2_V + hi * 256 + r32 * 16 : K2_ONES + hi * 256 + (r32 - 16) * 16);
            const int vinc1 = r32 < 16 ? 1024 : 0;
            float m = m0;
            o = zero;
#pragma unroll 1
            for (int t = 0; t < 64; ++t) {
                f32x16 s = MFMA(lds_rd128(kp), qf, zero);
                float tm = s[0];
#pragma unroll
                for (int r = 1; r < 16; ++r) tm = __builtin_fmaxf(tm, s[r]);
                tm = xhalf_max(tm);
                const float mn = __builtin_fmaxf(m, tm);
                const float al = __builtin_amdgcn_exp2f(m - mn);
                m = mn;
#pragma unroll
                for (int r = 0; r < 16; ++r) { o[r] *= al; s[r] = __builtin_amdgcn_exp2f(s[r] - mn); }
                o = MFMA(lds_rd128(vp), pack8n(s, 0), o);
                o = MFMA(lds_rd128(vp + 512), pack8n(s, 1), o);
                kp += 1024; vp += vinc1;
            }
        }
    }
    {
        const float f = (1.f - gate) / o[8];
        *(bf16x8*)(ws + WS_O + ((size_t)(qtile * 64 + 32 + head) * 64 + lane) * 16) = pack8(o, 0, f);
    }
    {
#pragma unroll
        for (int i = 0; i < 2; ++i) {
            float acc = 0.f;
#pragma unroll
            for (int p = 0; p < 16; ++p) acc += part[i][p];
            *(LAS u16*)(lds + K2_KV + (tid + 512 * i) * 2) = (u16)(cvtpk(acc, acc) & 0xffffu);
        }
        __syncthreads();
        f32x16 a = MFMA(lds_rd128(lds + K2_KV + lane * 16), phq0, zero);
        a = MFMA(lds_rd128(lds + K2_KV + 1024 + lane * 16), phq1, a);
        const float f = gate / (a[8] + 1e-6f);
        *(bf16x8*)(ws + WS_O + ((size_t)(qtile * 64 + head) * 64 + lane) * 16) = pack8(a, 0, f);
    }
    if (w < 4) {
        const int fr = b * 4 + w, nt = fr >> 6, ks = fr & 63;
        const u16* src = (ks < 32 ? woa : wob) + (size_t)((ks & 31) * 16 + 8 * hi) * DM + nt * 32 + r32;
        bf16x8 f;
#pragma unroll
        for (int j = 0; j < 8; ++j) f[j] = (short)src[j * DM];
        *(bf16x8*)(ws + WS_WO + ((size_t)fr * 64 + lane) * 16) = f;
    }
}

constexpr int K3_LDS = 8 * 16384;
__global__ __launch_bounds__(512, 2) void k3_proj(const char* __restrict__ ws, u16* __restrict__ out) {
    extern __shared__ __attribute__((aligned(16))) char smem[];
    const lds_ptr lds = (lds_ptr)smem;
    const int tid = threadIdx.x, lane = tid & 63;
    const int w = __builtin_amdgcn_readfirstlane(tid >> 6);
    const int b = blockIdx.x, xcd = b & 7, bi = b >> 3;
    const int mt = xcd * 4 + (bi >> 3), nt = bi & 7;
    bf16x8 of[2][8], wf[2][8];
#pragma unroll
    for (int k = 0; k < 8; ++k)
#pragma unroll
        for (int i = 0; i < 2; ++i) {
            wf[i][k] = *(const bf16x8*)(ws + WS_WO + ((size_t)((nt * 2 + i) * 64 + w * 8 + k) * 64 + lane) * 16);
            of[i][k] = *(const bf16x8*)(ws + WS_O + ((size_t)((mt * 2 + i) * 64 + w * 8 + k) * 64 + lane) * 16);
        }
    f32x16 acc[2][2] = {};
#pragma unroll
    for (int k = 0; k < 8; ++k)
#pragma unroll
        for (int i = 0; i < 2; ++i)
#pragma unroll
            for (int j = 0; j < 2; ++j) acc[i][j] = MFMA(wf[i][k], of[j][k], acc[i][j]);
#pragma unroll
    for (int i = 0; i < 2; ++i)
#pragma unroll
        for (int j = 0; j < 2; ++j)
#pragma unroll
            for (int g = 0; g < 4; ++g) {
                const f32x4 v = {acc[i][j][4 * g], acc[i][j][4 * g + 1], acc[i][j][4 * g + 2], acc[i][j][4 * g + 3]};
                *(LAS f32x4*)(lds + w * 16384 + (((i * 2 + j) * 4 + g) * 64 + lane) * 16) = v;
            }
    __syncthreads();
    {
        const int tk = tid >> 3, c = tid & 7, i = c >> 2, g = c & 3, j = tk >> 5, t32 = tk & 31;
        f32x4 s0 = {}, s1 = {};
#pragma unroll
        for (int ww = 0; ww < 8; ++ww) {
            const lds_ptr p = lds + ww * 16384 + (((i * 2 + j) * 4 + g) * 64 + t32) * 16;
            s0 += *(const LAS f32x4*)p;
            s1 += *(const LAS f32x4*)(p + 512);
        }
        u32x4 o;
        o[0] = cvtpk(s0[0], s0[1]); o[1] = cvtpk(s0[2], s0[3]); o[2] = cvtpk(s1[0], s1[1]); o[3] = cvtpk(s1[2], s1[3]);
        *(u32x4*)(out + (size_t)(mt * 64 + tk) * DM + nt * 64 + c * 8) = o;
    }
}

extern "C" void kernel_launch(void* const* d_in, const int* in_sizes, int n_in, void* d_out, int out_size, void* d_ws, size_t ws_size,
                              hipStream_t stream) {
    const u16* x = (const u16*)d_in[0];
    const u16* wa = (const u16*)d_in[1];
    const u16* wphi = (const u16*)d_in[2];
    const u16* woa = (const u16*)d_in[3];
    const u16* wb = (const u16*)d_in[5];
    const u16* wob = (const u16*)d_in[6];
    const u16* gw = (const u16*)d_in[7];
    char* ws = (char*)d_ws;
    (void)hipFuncSetAttribute((const void*)k1_qkv, hipFuncAttributeMaxDynamicSharedMemorySize, 4 * K1_SLOT);
    (void)hipFuncSetAttribute((const void*)k2_attn, hipFuncAttributeMaxDynamicSharedMemorySize, K2_LDS);
    (void)hipFuncSetAttribute((const void*)k3_proj, hipFuncAttributeMaxDynamicSharedMemorySize, K3_LDS);
    k1_qkv<<<256, 512, 4 * K1_SLOT, stream>>>(x, wa, wb, wphi, gw, ws);
    k2_attn<<<256, 512, K2_LDS, stream>>>(woa, wob, ws);
    k3_proj<<<256, 512, K3_LDS, stream>>>(ws, (u16*)d_out);
}
```

```cpp
#include <hip/hip_runtime.h>
#include <hip/hip_bf16.h>
#include <stdint.h>

typedef __attribute__((ext_vector_type(8))) short bf16x8;
typedef __attribute__((ext_vector_type(4))) short s16x4;
typedef __attribute__((ext_vector_type(16))) float f32x16;
typedef __attribute__((ext_vector_type(4))) float f32x4;
typedef __attribute__((ext_vector_type(2))) float f32x2;
typedef __attribute__((ext_vector_type(4))) unsigned u32x4;
typedef __attribute__((ext_vector_type(2))) unsigned u32x2;
typedef __attribute__((ext_vector_type(2))) __bf16 bf16x2_t;
typedef unsigned short u16;
#define LAS __attribute__((address_space(3)))
typedef LAS char* lds_ptr;
#define DI __device__ __forceinline__
#define MFMA(a, b, c) __builtin_amdgcn_mfma_f32_32x32x16_bf16((a), (b), (c), 0, 0, 0)
#define SBAR() __builtin_amdgcn_sched_barrier(0)

constexpr int DM = 512;
constexpr float LOG2E = 1.4426950408889634f;
constexpr float QSCALE = 0.25f * LOG2E;

constexpr unsigned WS_F = 0;
constexpr unsigned WS_R = WS_F + (128u << 10);
constexpr unsigned WS_G = WS_R + (4u << 10);
constexpr unsigned WS_Q = 1u << 20;
constexpr unsigned WS_KV = WS_Q + (2u << 20);
constexpr unsigned WS_PHIQ = WS_KV + (4u << 20);
constexpr unsigned WS_KVP = WS_PHIQ + (4u << 20);
constexpr unsigned WS_O = WS_KVP + (2u << 20);
constexpr unsigned WS_END = WS_O + (4u << 20);

DI unsigned cvtpk(float a, float b) {
    f32x2 v = {a, b};
    bf16x2_t r = __builtin_convertvector(v, bf16x2_t);
    return __builtin_bit_cast(unsigned, r);
}
DI float bf2f(u16 v) { return __uint_as_float((unsigned)v << 16); }
DI u32x4 pack8(const f32x16& x, int s, float sc) {
    u32x4 p;
    p[0] = cvtpk(x[8 * s + 0] * sc, x[8 * s + 1] * sc);
    p[1] = cvtpk(x[8 * s + 2] * sc, x[8 * s + 3] * sc);
    p[2] = cvtpk(x[8 * s + 4] * sc, x[8 * s + 5] * sc);
    p[3] = cvtpk(x[8 * s + 6] * sc, x[8 * s + 7] * sc);
    return p;
}
DI u32x4 pack8u(const f32x16& x, int s) {
    u32x4 p;
    p[0] = cvtpk(x[8 * s + 0], x[8 * s + 1]);
    p[1] = cvtpk(x[8 * s + 2], x[8 * s + 3]);
    p[2] = cvtpk(x[8 * s + 4], x[8 * s + 5]);
    p[3] = cvtpk(x[8 * s + 6], x[8 * s + 7]);
    return p;
}
#define BF8(x) __builtin_bit_cast(bf16x8, (x))
DI bf16x8 lds_rd128(lds_ptr p) { return *(const LAS bf16x8*)p; }
typedef short v4i16_t __attribute__((ext_vector_type(4)));
DI s16x4 lds_tr(lds_ptr p) { return __builtin_bit_cast(s16x4, __builtin_amdgcn_ds_read_tr16_b64_v4i16((LAS v4i16_t*)p)); }
DI bf16x8 cat8(s16x4 lo, s16x4 hi) { return __builtin_shufflevector(lo, hi, 0, 1, 2, 3, 4, 5, 6, 7); }
DI float xhalf_max(float m) {
    auto rr = __builtin_amdgcn_permlane32_swap(__float_as_uint(m), __float_as_uint(m), false, false);
    return __builtin_fmaxf(__uint_as_float(rr[0]), __uint_as_float(rr[1]));
}
DI float xhalf_sum(float m) {
    auto rr = __builtin_amdgcn_permlane32_swap(__float_as_uint(m), __float_as_uint(m), false, false);
    return __uint_as_float(rr[0]) + __uint_as_float(rr[1]);
}
DI void glds16(const void* base, unsigned off, unsigned lds_base) {
    asm volatile("s_mov_b32 m0, %2\n\ts_nop 0\n\tglobal_load_lds_dwordx4 %0, %1" :: "v"(off), "s"(base), "s"(lds_base) : "memory");
}
DI void glds16x2_dev(const void* base, unsigned off, unsigned lds_base) {
    asm volatile("s_mov_b32 m0, %2\n\ts_nop 0\n\tglobal_load_lds_dwordx4 %0, %1 sc1\n\tglobal_load_lds_dwordx4 %0, %1 offset:1024 sc1"
                 :: "v"(off), "s"(base), "s"(lds_base) : "memory");
}
#define WAIT_BAR(N) asm volatile("s_waitcnt vmcnt(" #N ") lgkmcnt(0)\n\ts_barrier" ::: "memory")
#define LBAR() asm volatile("s_waitcnt lgkmcnt(0)\n\ts_barrier" ::: "memory")
#define DRAIN() asm volatile("s_waitcnt vmcnt(0)" ::: "memory")
typedef __amdgpu_buffer_rsrc_t rsrc_t;
DI void st128(rsrc_t r, unsigned off, u32x4 v) { __builtin_amdgcn_raw_buffer_store_b128(v, r, off, 0, 16); }
DI void st32(rsrc_t r, unsigned off, unsigned v) { __builtin_amdgcn_raw_buffer_store_b32(v, r, off, 0, 16); }
DI u32x4 ld128(rsrc_t r, unsigned off) { return __builtin_amdgcn_raw_buffer_load_b128(r, off, 0, 16); }
DI unsigned ld32(rsrc_t r, unsigned off) {
    asm volatile("" ::: "memory");
    const unsigned v = __builtin_amdgcn_raw_buffer_load_b32(r, off, 0, 16);
    asm volatile("" ::: "memory");
    return v;
}

#define ATTN_ASM \
    "s_mov_b32 %[pre], 0\n\t" \
    "s_mov_b32 %[ph], 0\n\t" \
    "s_mov_b32 %[cnt], 3\n\t" \
    "s_waitcnt lgkmcnt(0)\n\t" \
    "s_barrier\n\t" \
    "ds_read_b128 v[80:83], %[ma]\n\t" \
    "ds_read_b128 v[84:87], %[mb]\n\t" \
    "ds_read_b128 v[152:155], %[oo]\n\t" \
    "ds_read_b128 v[156:159], %[kp]\n\t" \
    "ds_read_b128 v[164:167], %[vp] offset:512\n\t" \
    "ds_read_b128 v[160:163], %[vp]\n\t" \
    "s_waitcnt lgkmcnt(3)\n\t" \
    "v_mfma_f32_32x32x16_bf16 v[120:135], v[152:155], v[80:83], 0\n\t" \
    "v_mfma_f32_32x32x16_bf16 v[136:151], v[152:155], v[84:87], 0\n\t" \
    "s_nop 10\n\t" \
    "v_max3_f32 v120, v120, v121, v122\n\t" \
    "v_max3_f32 v123, v123, v124, v125\n\t" \
    "v_max3_f32 v126, v126, v127, v128\n\t" \
    "v_max3_f32 v129, v129, v130, v131\n\t" \
    "v_max3_f32 v132, v132, v133, v134\n\t" \
    "v_max3_f32 v120, v120, v123, v126\n\t" \
    "v_max3_f32 v129, v129, v132, v135\n\t" \
    "v_max_f32 v120, v120, v129\n\t" \
    "v_mov_b32 v121, v120\n\t" \
    "s_nop 1\n\t" \
    "v_permlane32_swap_b32 v120, v121\n\t" \
    "s_nop 1\n\t" \
    "v_max_f32 %[ma], v120, v121\n\t" \
    "v_sub_f32 v88, 0, %[ma]\n\t" \
    "v_sub_f32 v89, 0, %[ma]\n\t" \
    "v_pk_mov_b32 v[90:91], v[88:89], v[88:89]\n\t" \
    "v_pk_mov_b32 v[92:93], v[88:89], v[88:89]\n\t" \
    "v_pk_mov_b32 v[94:95], v[88:89], v[88:89]\n\t" \
    "v_pk_mov_b32 v[96:97], v[88:89], v[88:89]\n\t" \
    "v_pk_mov_b32 v[98:99], v[88:89], v[88:89]\n\t" \
    "v_pk_mov_b32 v[100:101], v[88:89], v[88:89]\n\t" \
    "v_pk_mov_b32 v[102:103], v[88:89], v[88:89]\n\t" \
    "s_waitcnt lgkmcnt(0)\n\t" \
    "s_nop 0\n\t" \
    "v_mfma_f32_32x32x16_bf16 v[120:135], v[156:159], v[80:83], v[88:103]\n\t" \
    "v_max3_f32 v136, v136, v137, v138\n\t" \
    "v_max3_f32 v139, v139, v140, v141\n\t" \
    "v_max3_f32 v142, v142, v143, v144\n\t" \
    "v_max3_f32 v145, v145, v146, v147\n\t" \
    "v_max3_f32 v148, v148, v149, v150\n\t" \
    "v_max3_f32 v136, v136, v139, v142\n\t" \
    "v_max3_f32 v145, v145, v148, v151\n\t" \
    "v_max_f32 v136, v136, v145\n\t" \
    "v_mov_b32 v137, v136\n\t" \
    "s_nop 1\n\t" \
    "v_permlane32_swap_b32 v136, v137\n\t" \
    "s_nop 1\n\t" \
    "v_max_f32 %[mb], v136, v137\n\t" \
    "v_sub_f32 v104, 0, %[mb]\n\t" \
    "v_sub_f32 v105, 0, %[mb]\n\t" \
    "v_pk_mov_b32 v[106:107], v[104:105], v[104:105]\n\t" \
    "v_pk_mov_b32 v[108:109], v[104:105], v[104:105]\n\t" \
    "v_pk_mov_b32 v[110:111], v[104:105], v[104:105]\n\t" \
    "v_pk_mov_b32 v[112:113], v[104:105], v[104:105]\n\t" \
    "v_pk_mov_b32 v[114:115], v[104:105], v[104:105]\n\t" \
    "v_pk_mov_b32 v[116:117], v[104:105], v[104:105]\n\t" \
    "v_pk_mov_b32 v[118:119], v[104:105], v[104:105]\n\t" \
    "v_mov_b32 v144, 0\n\t" \
    "v_mov_b32 v145, 0\n\t" \
    "v_mov_b32 v146, 0\n\t" \
    "v_mov_b32 v147, 0\n\t" \
    "v_mov_b32 v148, 0\n\t" \
    "v_mov_b32 v149, 0\n\t" \
    "v_mov_b32 v150, 0\n\t" \
    "v_mov_b32 v151, 0\n\t" \
    "1:\n\t" \
    "s_setprio 0\n\t" \
    "s_bitcmp1_b32 %[pat], %[cnt]\n\t" \
    "s_cbranch_scc0 5f\n\t" \
    "s_setprio 1\n\t" \
    "5:\n\t" \
    "v_exp_f32 v120, v120\n\t" \
    "v_exp_f32 v121, v121\n\t" \
    "v_smfmac_f32_32x32x32_bf16 %[ob], v[164:167], v[144:151], %[idx]\n\t" \
    "ds_read_b128 v[164:167], %[vp] offset:512\n\t" \
    "v_exp_f32 v122, v122\n\t" \
    "v_exp_f32 v123, v123\n\t" \
    "v_exp_f32 v124, v124\n\t" \
    "v_exp_f32 v125, v125\n\t" \
    "v_mfma_f32_32x32x16_bf16 v[136:151], v[156:159], v[84:87], v[104:119]\n\t" \
    "ds_read_b128 v[156:159], %[kp] offset:2048\n\t" \
    "v_exp_f32 v126, v126\n\t" \
    "v_exp_f32 v127, v127\n\t" \
    "v_exp_f32 v128, v128\n\t" \
    "v_exp_f32 v129, v129\n\t" \
    "v_exp_f32 v130, v130\n\t" \
    "s_waitcnt lgkmcnt(2)\n\t" \
    "v_smfmac_f32_32x32x32_bf16 %[oa], v[160:163], v[120:127], %[idx]\n\t" \
    "v_exp_f32 v131, v131\n\t" \
    "v_exp_f32 v132, v132\n\t" \
    "v_exp_f32 v133, v133\n\t" \
    "v_exp_f32 v134, v134\n\t" \
    "v_exp_f32 v135, v135\n\t" \
    "v_exp_f32 v136, v136\n\t" \
    "v_exp_f32 v137, v137\n\t" \
    "s_waitcnt lgkmcnt(0)\n\t" \
    "v_smfmac_f32_32x32x32_bf16 %[oa], v[164:167], v[128:135], %[idx]\n\t" \
    "v_exp_f32 v138, v138\n\t" \
    "v_exp_f32 v139, v139\n\t" \
    "v_exp_f32 v140, v140\n\t" \
    "v_exp_f32 v141, v141\n\t" \
    "v_mfma_f32_32x32x16_bf16 v[120:135], v[156:159], v[80:83], v[88:103]\n\t" \
    "v_exp_f32 v142, v142\n\t" \
    "v_exp_f32 v143, v143\n\t" \
    "v_exp_f32 v144, v144\n\t" \
    "v_exp_f32 v145, v145\n\t" \
    "v_exp_f32 v146, v146\n\t" \
    "v_smfmac_f32_32x32x32_bf16 %[ob], v[160:163], v[136:143], %[idx]\n\t" \
    "ds_read_b128 v[160:163], %[vp] offset:2048\n\t" \
    "v_exp_f32 v147, v147\n\t" \
    "v_exp_f32 v148, v148\n\t" \
    "v_exp_f32 v149, v149\n\t" \
    "v_exp_f32 v150, v150\n\t" \
    "v_exp_f32 v151, v151\n\t" \
    "v_exp_f32 v120, v120\n\t" \
    "v_exp_f32 v121, v121\n\t" \
    "v_smfmac_f32_32x32x32_bf16 %[ob], v[164:167], v[144:151], %[idx]\n\t" \
    "ds_read_b128 v[164:167], %[vp] offset:2560\n\t" \
    "v_exp_f32 v122, v122\n\t" \
    "v_exp_f32 v123, v123\n\t" \
    "v_exp_f32 v124, v124\n\t" \
    "v_exp_f32 v125, v125\n\t" \
    "v_mfma_f32_32x32x16_bf16 v[136:151], v[156:159], v[84:87], v[104:119]\n\t" \
    "ds_read_b128 v[156:159], %[kp] offset:4096\n\t" \
    "v_exp_f32 v126, v126\n\t" \
    "v_exp_f32 v127, v127\n\t" \
    "v_exp_f32 v128, v128\n\t" \
    "v_exp_f32 v129, v129\n\t" \
    "v_exp_f32 v130, v130\n\t" \
    "s_waitcnt lgkmcnt(2)\n\t" \
    "v_smfmac_f32_32x32x32_bf16 %[oa], v[160:163], v[120:127], %[idx]\n\t" \
    "v_exp_f32 v131, v131\n\t" \
    "v_exp_f32 v132, v132\n\t" \
    "v_exp_f32 v133, v133\n\t" \
    "v_exp_f32 v134, v134\n\t" \
    "v_exp_f32 v135, v135\n\t" \
    "v_exp_f32 v136, v136\n\t" \
    "v_exp_f32 v137, v137\n\t" \
    "s_waitcnt lgkmcnt(0)\n\t" \
    "v_smfmac_f32_32x32x32_bf16 %[oa], v[164:167], v[128:135], %[idx]\n\t" \
    "v_exp_f32 v138, v138\n\t" \
    "v_exp_f32 v139, v139\n\t" \
    "v_exp_f32 v140, v140\n\t" \
    "v_exp_f32 v141, v141\n\t" \
    "v_mfma_f32_32x32x16_bf16 v[120:135], v[156:159], v[80:83], v[88:103]\n\t" \
    "v_add_u32 %[vp], %[vp], %[vinc]\n\t" \
    "v_exp_f32 v142, v142\n\t" \
    "v_exp_f32 v143, v143\n\t" \
    "v_exp_f32 v144, v144\n\t" \
    "v_exp_f32 v145, v145\n\t" \
    "v_exp_f32 v146, v146\n\t" \
    "v_smfmac_f32_32x32x32_bf16 %[ob], v[160:163], v[136:143], %[idx]\n\t" \
    "ds_read_b128 v[160:163], %[vp] offset:0\n\t" \
    "v_add_u32 %[kp], 4096, %[kp]\n\t" \
    "s_sub_u32 %[cnt], %[cnt], 1\n\t" \
    "v_exp_f32 v147, v147\n\t" \
    "v_exp_f32 v148, v148\n\t" \
    "v_exp_f32 v149, v149\n\t" \
    "v_exp_f32 v150, v150\n\t" \
    "v_exp_f32 v151, v151\n\t" \
    "s_cmp_lg_u32 %[cnt], 0\n\t" \
    "s_cbranch_scc1 1b\n\t" \
    "s_add_u32 %[ph], %[ph], 1\n\t" \
    "s_cmp_eq_u32 %[ph], 5\n\t" \
    "s_cbranch_scc1 2f\n\t" \
    "s_lshl_b32 %[cnt], %[ph], 2\n\t" \
    "s_lshr_b32 %[cnt], 0x26143, %[cnt]\n\t" \
    "s_and_b32 %[cnt], %[cnt], 15\n\t" \
    "s_cmp_lt_u32 %[ph], 3\n\t" \
    "s_cbranch_scc1 3f\n\t" \
    "v_mov_b32 v152, %[ph]\n\t" \
    "ds_write_b32 %[prog], v152\n\t" \
    "s_cmp_lg_u32 %[ph], 4\n\t" \
    "s_cbranch_scc1 1b\n\t" \
    "ds_read_b32 v153, %[cb] offset:12\n\t" \
    "s_waitcnt lgkmcnt(0)\n\t" \
    "v_readfirstlane_b32 %[pre], v153\n\t" \
    "s_nop 3\n\t" \
    "s_cmp_lt_u32 %[pre], 4\n\t" \
    "s_cbranch_scc1 1b\n\t" \
    "global_load_dwordx4 %[f0], %[oo], %[ws0] offset:0 sc1\n\t" \
    "global_load_dwordx4 %[f1], %[oo], %[ws0] offset:1024 sc1\n\t" \
    "global_load_dwordx4 %[f2], %[oo], %[ws0] offset:2048 sc1\n\t" \
    "global_load_dwordx4 %[f3], %[oo], %[ws0] offset:3072 sc1\n\t" \
    "global_load_dwordx4 %[f4], %[oo], %[ws1] offset:0 sc1\n\t" \
    "global_load_dwordx4 %[f5], %[oo], %[ws1] offset:1024 sc1\n\t" \
    "global_load_dwordx4 %[f6], %[oo], %[ws1] offset:2048 sc1\n\t" \
    "global_load_dwordx4 %[f7], %[oo], %[ws1] offset:3072 sc1\n\t" \
    "s_branch 1b\n\t" \
    "3:\n\t" \
    "ds_read_b128 v[152:155], %[cb] offset:16\n\t" \
    "s_waitcnt lgkmcnt(0)\n\t" \
    "v_min3_u32 v152, v152, v153, v154\n\t" \
    "v_min_u32 v152, v152, v155\n\t" \
    "s_nop 0\n\t" \
    "v_cmp_gt_u32 vcc, %[ph], v152\n\t" \
    "s_nop 4\n\t" \
    "s_cbranch_vccz 1b\n\t" \
    "s_sleep 1\n\t" \
    "s_branch 3b\n\t" \
    "2:\n\t" \
    "s_setprio 0\n\t" \
    "s_waitcnt lgkmcnt(0)\n\t" \
    "v_smfmac_f32_32x32x32_bf16 %[ob], v[164:167], v[144:151], %[idx]\n\t" \
    "s_waitcnt vmcnt(0)\n\t" \
    "s_nop 11\n\t"
#define ATTN_CLOB \
    "v80", "v81", "v82", "v83", "v84", "v85", "v86", "v87", "v88", "v89", "v90", "v91", "v92", "v93", "v94", "v95", \
    "v96", "v97", "v98", "v99", "v100", "v101", "v102", "v103", "v104", "v105", "v106", "v107", "v108", "v109", "v110", "v111", \
    "v112", "v113", "v114", "v115", "v116", "v117", "v118", "v119", "v120", "v121", "v122", "v123", "v124", "v125", "v126", "v127", \
    "v128", "v129", "v130", "v131", "v132", "v133", "v134", "v135", "v136", "v137", "v138", "v139", "v140", "v141", "v142", "v143", \
    "v144", "v145", "v146", "v147", "v148", "v149", "v150", "v151", "v152", "v153", "v154", "v155", "v156", "v157", "v158", "v159", \
    "v160", "v161", "v162", "v163", "v164", "v165", "v166", "v167"

constexpr int K1_SLOT = 40960, K1_WOFF = 16384;
constexpr int L_E1 = 36848, L_WPHI = 36864, L_VA = 49152, L_TIMG = 98304;
constexpr int L_ONES = 131072, L_GATE = L_ONES + 1024, L_CNT = L_ONES + 3072;
constexpr int L_LAND = L_CNT + 16, L_PROG = L_CNT + 32, L_PAIR = L_CNT + 64;
constexpr int L_MASK = L_CNT + 80, L_WOB = L_CNT + 84, L_ADONE = L_CNT + 88, L_KVD = L_CNT + 96, L_KVS = L_CNT + 100;
constexpr int L_RED2 = L_CNT + 256, L_KVF = L_RED2 + 18432, L_Q = L_KVF + 2048;
constexpr int LDS_BYTES = 163840;
static_assert(L_Q + 8192 <= LDS_BYTES, "LDS map");
constexpr int SPIN_CAP = 200000;
constexpr unsigned EMPTY = 0xFFFFFFFFu;
constexpr unsigned CODE_STRIDE = 88u;
constexpr unsigned PRIO_PAT = 0xFFFBu;

DI void feat_softmax(f32x16& p, int s) {
    {
        float m = __builtin_fmaxf(__builtin_fmaxf(p[8 * s], p[8 * s + 1]), p[8 * s + 2]);
        m = __builtin_fmaxf(__builtin_fmaxf(m, p[8 * s + 3]), p[8 * s + 4]);
        m = __builtin_fmaxf(__builtin_fmaxf(m, p[8 * s + 5]), p[8 * s + 6]);
        m = xhalf_max(__builtin_fmaxf(m, p[8 * s + 7]));
        const float nm = -m * LOG2E;
#pragma unroll
        for (int j = 0; j < 8; ++j) p[8 * s + j] = __builtin_amdgcn_exp2f(__builtin_fmaf(p[8 * s + j], LOG2E, nm));
        const float sum = ((p[8 * s] + p[8 * s + 1]) + (p[8 * s + 2] + p[8 * s + 3])) + ((p[8 * s + 4] + p[8 * s + 5]) + (p[8 * s + 6] + p[8 * s + 7]));
        const float r = __builtin_amdgcn_rcpf(xhalf_sum(sum));
#pragma unroll
        for (int j = 0; j < 8; ++j) p[8 * s + j] *= r;
    }
}
typedef __attribute__((ext_vector_type(8))) __bf16 bf16v8;
typedef __attribute__((ext_vector_type(16))) __bf16 bf16v16;
typedef __attribute__((ext_vector_type(8))) float f32x8;
DI f32x16 pv_sparse(u32x4 v, const f32x16& p, int s, f32x16 o) {
    const f32x8 b = {p[8 * s], p[8 * s + 1], p[8 * s + 2], p[8 * s + 3], p[8 * s + 4], p[8 * s + 5], p[8 * s + 6], p[8 * s + 7]};
    return __builtin_amdgcn_smfmac_f32_32x32x32_bf16(__builtin_bit_cast(bf16v8, v), __builtin_bit_cast(bf16v16, b), o, 0xDDDD, 0, 0);
}
DI unsigned lds_ld(lds_ptr p) { return __hip_atomic_load((LAS unsigned*)p, __ATOMIC_RELAXED, __HIP_MEMORY_SCOPE_WORKGROUP); }
DI void lds_arrive(unsigned addr) {
    asm volatile("s_waitcnt vmcnt(0) lgkmcnt(0)\n\ts_mov_b64 exec, 1\n\tds_add_u32 %0, %1\n\ts_mov_b64 exec, -1" :: "v"(addr), "v"(1u) : "memory");
}
DI void lds_arrive_l(unsigned addr) {
    asm volatile("s_waitcnt lgkmcnt(0)\n\ts_mov_b64 exec, 1\n\tds_add_u32 %0, %1\n\ts_mov_b64 exec, -1" :: "v"(addr), "v"(1u) : "memory");
}
DI void lds_await(lds_ptr c, unsigned v) {
#pragma nounroll
    for (int spin = 0; spin < 64 * SPIN_CAP && lds_ld(c) < v; ++spin) __builtin_amdgcn_s_sleep(1);
    asm volatile("" ::: "memory");
}

__global__ __launch_bounds__(768, 3) void fused_kernel(const u16* __restrict__ x, const u16* __restrict__ wa, const u16* __restrict__ wb,
                                                        const u16* __restrict__ wphi, const u16* __restrict__ gw, const u16* __restrict__ woa,
                                                        const u16* __restrict__ wob, char* ws, u16* __restrict__ out) {
    extern __shared__ __attribute__((aligned(16))) char smem[];
    const lds_ptr lds = (lds_ptr)smem;
    const unsigned lds0 = (unsigned)(uintptr_t)smem;
    const int tid = threadIdx.x, lane = tid & 63, r32 = lane & 31, hi = lane >> 5;
    const int w = __builtin_amdgcn_readfirstlane(tid >> 6);
    unsigned code_touch;
    { unsigned long long pc0;
      asm volatile("s_getpc_b64 %1\n\tglobal_load_dword %0, %2, %1" : "=&v"(code_touch), "=&s"(pc0) : "v"(tid < 64 ? (((blockIdx.x >> 3) & 3u) * 64u + (unsigned)tid) * CODE_STRIDE : 0u) : "memory"); }
    const int b = blockIdx.x, xcd = b & 7, bi = b >> 3;
    const int tb = (xcd & 1) * 8 + (bi & 7);
    const int combo = (xcd >> 1) * 4 + (bi >> 3);
    const int hq = combo >> 1, br = combo & 1;
    const int head = 4 * hq + 2 * (xcd & 1) + br, qb = bi & 7;
    const int id = head * 8 + qb;
    const int blk0 = (xcd & 1) * 8 + (qb & 1) * 4;
    const int mt = qb * 4 + (xcd >> 1), nt = (xcd & 1) * 4 + (bi >> 3);
    const rsrc_t rs = __builtin_amdgcn_make_buffer_rsrc(ws, 0, WS_END, 0x00020000);
    const f32x16 zero = {};
    asm volatile("" :: "s"(x), "s"(wa), "s"(wb), "s"(wphi), "s"(gw), "s"(woa), "s"(wob), "s"(ws), "s"(out));

    if (w < 8) {
    const int g16 = lane >> 4, q4 = (lane & 15) >> 2, p4 = lane & 3;
    {
        const int sub = w & 3, hp = w >> 2;
        unsigned nf0 = 0, nf1 = 0, nr = 0;
        const int xrow = 32 * sub + r32;
        const int xsw = (xrow >> 1) & 7;
        const unsigned wrd = K1_WOFF + (8 * hi + q4) * 128 + ((hp ^ ((q4 >> 1) & 1)) * 64) + (g16 & 1) * 32 + p4 * 8;
        const int h0 = 4 * hq + 2 * hp;
        f32x16 aq = {}, ak = {}, av = {};
        bf16x8 xf, wq, wk, wv;
        LBAR();
        xf = lds_rd128(lds + xrow * 128 + ((hi ^ xsw) * 16));
        wq = cat8(lds_tr(lds + wrd), lds_tr(lds + wrd + 512));
        wk = cat8(lds_tr(lds + wrd + 8192), lds_tr(lds + wrd + 8192 + 512));
        wv = cat8(lds_tr(lds + wrd + 16384), lds_tr(lds + wrd + 16384 + 512));
        if (w == 0)
            asm volatile("buffer_load_dword %0, %3, %5, 0 offen sc1\n\tbuffer_load_dword %1, %3, %5, 0 offen offset:256 sc1\n\tbuffer_load_dword %2, %4, %5, 0 offen sc1"
                                 : "+v"(nf0), "+v"(nf1), "+v"(nr) : "v"(WS_F + (unsigned)id * 512u + (unsigned)lane * 4u), "v"(WS_R + (unsigned)id * 4u), "s"(rs) : "memory");
#pragma unroll
        for (int s = 0; s < 32; ++s) {
            const bf16x8 cx = xf, cq = wq, ck = wk, cv = wv;
            if ((s & 3) == 3 && s < 31) {
                asm volatile("" :: "v"(cx), "v"(cq), "v"(ck), "v"(cv));
                if (s == 7 && w == 0) {
                    asm volatile("s_waitcnt vmcnt(0)" : "+v"(nf0), "+v"(nf1), "+v"(nr) :: "memory");
                    unsigned n = __builtin_amdgcn_readfirstlane(nr) + 1u;
                    while (__ballot(nf0 == n || nf1 == n) != 0ull) ++n;
                    if (lane == 0) st32(rs, WS_R + (unsigned)id * 4u, n);
                }
                if (s == 19) { DRAIN(); asm volatile("" :: "v"(code_touch)); }
                LBAR();
            }
            const int sn = s < 31 ? s + 1 : s;
            const lds_ptr sl = lds + ((sn >> 2) & 3) * K1_SLOT;
            const int ks = sn & 3;
            const lds_ptr wp = sl + wrd + ks * 2048;
            SBAR();
            aq = MFMA(cq, cx, aq);
            asm volatile("" : "+v"(aq));
            SBAR();
            if (s < 31) { xf = lds_rd128(sl + xrow * 128 + (((2 * ks + hi) ^ xsw) * 16)); wq = cat8(lds_tr(wp), lds_tr(wp + 512)); }
            SBAR();
            ak = MFMA(ck, cx, ak);
            asm volatile("" : "+v"(ak));
            SBAR();
            if (s < 31) wk = cat8(lds_tr(wp + 8192), lds_tr(wp + 8192 + 512));
            SBAR();
            av = MFMA(cx, cv, av);
            asm volatile("" : "+v"(av));
            SBAR();
            if (s < 31) wv = cat8(lds_tr(wp + 16384), lds_tr(wp + 16384 + 512));
            SBAR();
        }
        __builtin_amdgcn_s_setprio(2);
        const int tile = tb * 4 + sub;
        const int d = r32 & 15;
        const int vrow = (d & 3) + 8 * ((d >> 2) & 1) + 4 * (d >> 3);
        const unsigned t0 = (unsigned)(h0 * 64 + tile);
        const u32x4 vf0 = pack8u(av, 0), vf1 = pack8u(av, 1);
        u32x4 fi0, fi1;
        if (br == 0) {
            const unsigned vo = WS_KV + (t0 + (unsigned)(r32 >> 4) * 64u) * 2048u + 1024u + (unsigned)(hi * 256 + vrow * 16);
            st128(rs, WS_KV + t0 * 2048u + (unsigned)lane * 16u, pack8u(ak, 0));
            st128(rs, WS_KV + (t0 + 64) * 2048u + (unsigned)lane * 16u, pack8u(ak, 1));
            {
                auto a0 = __builtin_amdgcn_permlane32_swap(vf0[0], vf0[2], false, false), a1 = __builtin_amdgcn_permlane32_swap(vf0[1], vf0[3], false, false);
                auto b0 = __builtin_amdgcn_permlane32_swap(vf1[0], vf1[2], false, false), b1 = __builtin_amdgcn_permlane32_swap(vf1[1], vf1[3], false, false);
                st128(rs, vo, (u32x4){a0[0], a1[0], a0[1], a1[1]});
                st128(rs, vo + 512u, (u32x4){b0[0], b1[0], b0[1], b1[1]});
            }
            fi0 = pack8u(aq, 0); fi1 = pack8u(aq, 1);
        } else {
            st128(rs, WS_Q + t0 * 1024u + (unsigned)lane * 16u, pack8(aq, 0, QSCALE));
            st128(rs, WS_Q + (t0 + 64) * 1024u + (unsigned)lane * 16u, pack8(aq, 1, QSCALE));
            fi0 = pack8u(ak, 0); fi1 = pack8u(ak, 1);
        }
        if (!hp) __builtin_amdgcn_s_setprio(0);
        u32x4 ph[4];
        {
            const unsigned sgn = (r32 & 16) ? 0x80008000u : 0u;
            const lds_ptr wp = lds + L_WPHI + hp * 1024 + (4 * hi + q4) * 32 + p4 * 8;
            u32x4 wc0 = __builtin_bit_cast(u32x4, cat8(lds_tr(wp), lds_tr(wp + 256)));
            u32x4 wc1 = __builtin_bit_cast(u32x4, cat8(lds_tr(wp + 512), lds_tr(wp + 768)));
#pragma unroll
            for (int j = 0; j < 4; ++j) { wc0[j] ^= sgn; wc1[j] ^= sgn; }
            f32x16 p0 = MFMA(BF8(wc0), BF8(fi0), zero);
            f32x16 p1 = MFMA(BF8(wc1), BF8(fi1), zero);
            feat_softmax(p0, 0);
            ph[0] = pack8u(p0, 0);
            asm volatile("" : "+v"(ph[0]));
            lds_arrive(lds0 + L_E1);
            __builtin_amdgcn_s_setprio(0);
            feat_softmax(p0, 1);
            feat_softmax(p1, 0);
            feat_softmax(p1, 1);
            ph[1] = pack8u(p0, 1); ph[2] = pack8u(p1, 0); ph[3] = pack8u(p1, 1);
        }
        lds_await(lds + L_E1, 8u);
        if (br == 0) {
#pragma unroll
            for (int i = 0; i < 4; ++i) *(LAS u32x4*)(lds + L_TIMG + (w * 4 + i) * 1024 + lane * 16) = ph[i];
        } else {
#pragma unroll
            for (int e = 0; e < 2; ++e) {
                const lds_ptr tw = lds + L_TIMG + (w * 2 + e) * 2048 + r32 * 64 + 8 * (hi ^ ((r32 >> 3) & 1));
                const int cs = ((r32 >> 1) & 3) * 16;
                *(LAS u32x2*)(tw + cs) = (u32x2){ph[2 * e][0], ph[2 * e][1]};
                *(LAS u32x2*)(tw + (cs ^ 16)) = (u32x2){ph[2 * e][2], ph[2 * e][3]};
                *(LAS u32x2*)(tw + (cs ^ 32)) = (u32x2){ph[2 * e + 1][0], ph[2 * e + 1][1]};
                *(LAS u32x2*)(tw + (cs ^ 48)) = (u32x2){ph[2 * e + 1][2], ph[2 * e + 1][3]};
            }
            const lds_ptr vw = lds + L_VA + w * 2048 + (hi * 32 + (r32 & 16) + vrow) * 16;
            *(LAS u32x4*)vw = vf0;
            *(LAS u32x4*)(vw + 1024) = vf1;
        }
    }
    const int w4 = w & 3, kh = w >> 2;
    const int j = w & 1, kq = w >> 1;
    float ma, mb;
    f32x16 oa = {}, ob = {};
    u32x4 of[8];
    unsigned pre;
    {
        unsigned kp = lds0 + lane * 16 + kh * 65536;
        unsigned vp = lds0 + (r32 < 16 ? kh * 65536 + 1024 + hi * 256 + r32 * 16 : L_ONES + hi * 256 + (r32 - 16) * 16);
        const unsigned vinc = r32 < 16 ? 4096 : 0;
        if (tid < 384) {
            const int slot = (tid >> 1) & 15;
            const unsigned val = (slot == 0 || slot == 4) ? 0x3F803F80u : 0u;
            *(LAS u32x2*)(lds + L_ONES + tid * 8) = (u32x2){val, val};
        } else if (tid < 416) *(LAS unsigned*)(lds + L_CNT + (tid - 384) * 4) = 0u;
        {
            unsigned cnt, ph;
            ma = __uint_as_float(lds0 + L_Q + w4 * 2048 + kh * 1024 + lane * 16);
            mb = __uint_as_float(lds0 + L_Q + w4 * 2048 + (kh ^ 1) * 1024 + lane * 16);
            asm volatile(ATTN_ASM
                : [oa] "+{v[16:31]}"(oa), [ob] "+{v[32:47]}"(ob), [kp] "+v"(kp), [vp] "+v"(vp), [cnt] "=&s"(cnt), [ph] "=&s"(ph), [pre] "=&s"(pre),
                  [ma] "+v"(ma), [mb] "+v"(mb),
                  [f0] "=&{v[48:51]}"(of[0]), [f1] "=&{v[52:55]}"(of[1]), [f2] "=&{v[56:59]}"(of[2]), [f3] "=&{v[60:63]}"(of[3]), [f4] "=&{v[64:67]}"(of[4]), [f5] "=&{v[68:71]}"(of[5]), [f6] "=&{v[72:75]}"(of[6]), [f7] "=&{v[76:79]}"(of[7])
                : [vinc] "v"(vinc), [cb] "v"(lds0 + L_CNT), [prog] "v"(lds0 + L_PROG + w * 4),
                  [pat] "s"(kh ? PRIO_PAT : 0u), [idx] "v"(0xDDDDu), [oo] "v"(lds0 + (unsigned)lane * 16u),
                  [ws0] "s"(ws + WS_O + ((mt * 2 + j) * 64 + kq * 8) * 1024 - lds0), [ws1] "s"(ws + WS_O + ((mt * 2 + j) * 64 + kq * 8 + 4) * 1024 - lds0)
                : "memory", "scc", "vcc", ATTN_CLOB);
        }
    }
    {
        int lane;
        asm volatile("v_mbcnt_lo_u32_b32 %0, -1, 0\n\tv_mbcnt_hi_u32_b32 %0, -1, %0" : "=v"(lane));
        const int r32 = lane & 31, hi = lane >> 5, g16 = lane >> 4, q4 = (lane & 15) >> 2, p4 = lane & 3;
        const unsigned oo = WS_O + (unsigned)((mt * 2 + j) * 64 + kq * 8) * 1024u + (unsigned)lane * 16u;
        const unsigned mya = lds0 + L_RED2 + w * 2304 + lane * 16, pra = lds0 + L_RED2 + (w ^ 4) * 2304 + lane * 16;
        const unsigned gta = lds0 + L_GATE + ((2 * w4 + kh) * 32 + r32) * 4;
        const f32x4 g0 = {ob[0], ob[1], ob[2], ob[3]}, g1 = {ob[4], ob[5], ob[6], ob[7]};
        const float gl = ob[8];
        f32x16 o = oa;
        unsigned ord, rdy, msk;
        f32x4 r0, r1;
        float rl, gq;
        asm volatile("ds_write_b128 %[mya], %[g0]\n\tds_write_b128 %[mya], %[g1] offset:1024\n\tds_write_b32 %[myl], %[gl] offset:2048\n\t"
                     "s_mov_b64 exec, 1\n\tds_write_b32 %[prog], %[five]\n\tds_add_rtn_u32 %[ord], %[pair], %[one]\n\tds_read_b32 %[rdy], %[cb] offset:12\n\t"
                     "ds_read_b32 %[msk], %[cb] offset:80\n\ts_mov_b64 exec, -1\n\t"
                     "ds_read_b128 %[r0], %[pra]\n\tds_read_b128 %[r1], %[pra] offset:1024\n\tds_read_b32 %[rl], %[prl] offset:2048\n\tds_read_b32 %[gq], %[gta]\n\t"
                     "s_waitcnt lgkmcnt(0)"
                     : [ord] "=&v"(ord), [rdy] "=&v"(rdy), [msk] "=&v"(msk), [r0] "=&v"(r0), [r1] "=&v"(r1), [rl] "=&v"(rl), [gq] "=&v"(gq)
                     : [mya] "v"(mya), [myl] "v"(mya - lane * 12), [g0] "v"(g0), [g1] "v"(g1), [gl] "v"(gl), [prog] "v"(lds0 + L_PROG + w * 4), [five] "v"(5u),
                       [pair] "v"(lds0 + L_PAIR + w4 * 4), [one] "v"(1u), [cb] "v"(lds0 + L_CNT), [pra] "v"(pra), [prl] "v"(pra - lane * 12), [gta] "v"(gta) : "memory");
        if (__builtin_amdgcn_readfirstlane(ord) == 0u || __builtin_amdgcn_readfirstlane(rdy) < 4u) {
            lds_await(lds + L_PAIR + w4 * 4, 2u);
            lds_await(lds + L_CNT + 12, 4u);
            r0 = *(const LAS f32x4*)(uintptr_t)pra; r1 = *(const LAS f32x4*)(uintptr_t)(pra + 1024);
            rl = *(const LAS float*)(uintptr_t)(pra - lane * 12 + 2048);
            gq = *(const LAS float*)(uintptr_t)gta;
            msk = lds_ld(lds + L_MASK);
        }
        {
#pragma unroll
            for (int r = 0; r < 4; ++r) { o[r] += r0[r]; o[4 + r] += r1[r]; }
            o[8] += rl;
            const float asum = ((o[0] + o[2]) + (o[4] + o[6])) + ((o[1] + o[3]) + (o[5] + o[7]));
            const bool ok = (__builtin_fabsf(asum) < 1.0e38f) && (o[8] < 1.0e30f) && (o[8] >= 0.5f);
            if (__builtin_expect(!__all(ok), 0)) {
                const unsigned ko = WS_KV + (unsigned)head * 131072u;
                const unsigned ones = (r32 == 16 || r32 == 20) ? 0x3F803F80u : 0u;
                const bf16x8 qf = lds_rd128(lds + L_Q + w4 * 2048 + kh * 1024 + lane * 16);
                float m = -3.0e38f;
                o = zero;
#pragma unroll 1
                for (int t = 0; t < 64; ++t) {
                    const u32x4 kf = ld128(rs, ko + (unsigned)t * 2048u + (unsigned)lane * 16u);
                    u32x4 v0 = ld128(rs, ko + (unsigned)t * 2048u + 1024u + (unsigned)(hi * 256 + (r32 & 15) * 16));
                    u32x4 v1 = ld128(rs, ko + (unsigned)t * 2048u + 1536u + (unsigned)(hi * 256 + (r32 & 15) * 16));
                    if (r32 >= 16) { v0 = (u32x4){ones, ones, ones, ones}; v1 = v0; }
                    f32x16 s = MFMA(BF8(kf), qf, zero);
                    float tm = s[0];
#pragma unroll
                    for (int r = 1; r < 16; ++r) tm = __builtin_fmaxf(tm, s[r]);
                    tm = xhalf_max(tm);
                    const float mn = __builtin_fmaxf(m, tm);
                    const float al = __builtin_amdgcn_exp2f(m - mn);
                    m = mn;
#pragma unroll
                    for (int r = 0; r < 16; ++r) { o[r] *= al; s[r] = __builtin_amdgcn_exp2f(s[r] - mn); }
                    o = pv_sparse(v0, s, 0, o);
                    o = pv_sparse(v1, s, 1, o);
                }
            }
            u32x4 da = pack8(o, 0, (1.f - gq) * __builtin_amdgcn_rcpf(o[8]));
#pragma unroll
            for (int i = 0; i < 4; ++i) da[i] = min(da[i], EMPTY - 1u);
            st128(rs, WS_O + (unsigned)((qb * 8 + 2 * w4 + kh) * 64 + 32 + head) * 1024u + (unsigned)lane * 16u, da);
        }
        if (pre < 4u) {
#pragma unroll
            for (int i = 0; i < 8; ++i) of[i] = ld128(rs, oo + (unsigned)i * 1024u);
            DRAIN();
#pragma unroll
            for (int i = 0; i < 8; ++i) asm volatile("" : "+v"(of[i]));
        }
        const bool early = (((__builtin_amdgcn_readfirstlane(msk) | (1u << head)) >> (8 * kq)) & 0xFFu) == 0xFFu;
        u32x4 og[8];
        if (early) {
#pragma unroll
            for (int i = 0; i < 8; ++i) og[i] = ld128(rs, oo + 32768u + (unsigned)i * 1024u);
        }
        const lds_ptr wp0 = lds + (kq >> 1) * 65536 + (kq & 1) * 16384 + (8 * hi + q4) * 128 + (g16 & 1) * 32 + p4 * 8;
        const int sw64 = ((q4 >> 1) & 1) * 64;
        f32x16 acc0 = {}, acc1 = {};
        lds_await(lds + L_KVD, 4u);
#pragma unroll
        for (int i = 0; i < 8; ++i) {
            const lds_ptr wp = wp0 + i * 2048;
            acc0 = MFMA(cat8(lds_tr(wp + sw64), lds_tr(wp + sw64 + 512)), BF8(of[i]), acc0);
            acc1 = MFMA(cat8(lds_tr(wp + (64 - sw64)), lds_tr(wp + (64 - sw64) + 512)), BF8(of[i]), acc1);
        }
        {
            const float g = *(const LAS float*)(lds + L_GATE + ((mt & 3) * 64 + j * 32 + r32) * 4);
#pragma unroll
            for (int r = 0; r < 16; ++r) { acc0[r] *= g; acc1[r] *= g; }
        }
        lds_arrive_l(lds0 + L_ADONE);
#pragma nounroll
        for (int spin = 0; spin < 64 * SPIN_CAP; ++spin) {
            const u32x2 mw = *(volatile LAS u32x2*)(lds + L_MASK);
            if ((early || ((mw[0] >> (8 * kq)) & 0xFFu) == 0xFFu) && mw[1] >= 4u) break;
            __builtin_amdgcn_s_sleep(1);
        }
        asm volatile("" ::: "memory");
        bf16x8 wf[16];
#pragma nounroll
        for (int tries = 0;; ++tries) {
            if (tries || !early) {
#pragma unroll
                for (int i = 0; i < 8; ++i) og[i] = ld128(rs, oo + 32768u + (unsigned)i * 1024u);
            }
            if (tries == 0) {
#pragma unroll
                for (int i = 0; i < 8; ++i) {
                    const lds_ptr wp = wp0 + 32768 + i * 2048;
                    wf[2 * i] = cat8(lds_tr(wp + sw64), lds_tr(wp + sw64 + 512));
                    wf[2 * i + 1] = cat8(lds_tr(wp + (64 - sw64)), lds_tr(wp + (64 - sw64) + 512));
                }
            }
            unsigned mx = 0u;
#pragma unroll
            for (int i = 0; i < 8; ++i) mx = max(max(mx, max(og[i][0], og[i][1])), max(og[i][2], og[i][3]));
            if (__ballot(mx == EMPTY) == 0ull || tries > SPIN_CAP) break;
            if (tries > 4) __builtin_amdgcn_s_sleep(32);
            asm volatile("" ::: "memory");
        }
#pragma unroll
        for (int i = 0; i < 8; ++i) {
            acc0 = MFMA(wf[2 * i], BF8(og[i]), acc0);
            acc1 = MFMA(wf[2 * i + 1], BF8(og[i]), acc1);
        }
        lds_await(lds + L_ADONE, 8u);
        const lds_ptr img = lds + j * 65536;
#pragma unroll
        for (int p = 0; p < 4; ++p) {
            const f32x16& a = (p >> 1) ? acc1 : acc0;
            const int q0 = (p & 1) * 8;
            LAS f32x4* dst = (LAS f32x4*)(img + ((p * 4 + kq) * 2) * 1024 + lane * 16);
            dst[0] = (f32x4){a[q0], a[q0 + 1], a[q0 + 2], a[q0 + 3]};
            dst[64] = (f32x4){a[q0 + 4], a[q0 + 5], a[q0 + 6], a[q0 + 7]};
        }
        LBAR();
        f32x4 s0 = {}, s1 = {};
#pragma unroll
        for (int q = 0; q < 4; ++q) {
            const LAS f32x4* sp = (const LAS f32x4*)(img + ((kq * 4 + q) * 2) * 1024 + lane * 16);
            s0 += sp[0]; s1 += sp[64];
        }
        const unsigned a0 = cvtpk(s0[0], s0[1]), a1 = cvtpk(s0[2], s0[3]), b0 = cvtpk(s1[0], s1[1]), b1 = cvtpk(s1[2], s1[3]);
        auto x0 = __builtin_amdgcn_permlane32_swap(a0, b0, false, false);
        auto x1 = __builtin_amdgcn_permlane32_swap(a1, b1, false, false);
        *(u32x4*)(out + (size_t)(mt * 64 + j * 32 + r32) * DM + nt * 64 + kq * 16 + hi * 8) = (u32x4){x0[0], x1[0], x0[1], x1[1]};
    }
    return;
    }
    {
        __builtin_amdgcn_s_setprio(3);
        const int sw = w - 8, st = tid - 512;
        const int l3 = lane >> 3;
        const unsigned l16 = (unsigned)lane * 16u;
        const unsigned swz1 = (unsigned)(((lane & 7) ^ (((l3 >> 1) & 1) << 2)) * 16);
        {
            const int rot = ((bi & 7) + 2 * (bi >> 3)) & 7;
            const char* xb = (const char*)x + (size_t)(tb * 128 + 32 * sw) * (DM * 2);
            const char* wbp = (const char*)(br ? wa : wb) + (size_t)(16 * sw * 1536 + hq * 64) * 2;
            const char* wqp = (const char*)(br ? wb : wa) + (size_t)(16 * sw * 1536 + hq * 64) * 2;
            const unsigned xo0 = (unsigned)(l3 * DM * 2) + (unsigned)(((lane & 7) ^ (l3 >> 1)) * 16);
            const unsigned xo1 = xo0 ^ 64u;
            const unsigned wo0 = (unsigned)(l3 * 1536 * 2) + swz1;
#pragma unroll 1
            for (int c = 0; c < 10; ++c) {
                if (c >= 2 && c <= 7) WAIT_BAR(10);
                else if (c == 8) {
                    if (st < 2) *(LAS unsigned*)(lds + L_E1 + st * 4) = 0u;
                    asm volatile("s_waitcnt vmcnt(10)" ::: "memory");
                    if (sw < 2) glds16((const char*)wphi + hq * 2048 + sw * 1024, l16, lds0 + L_WPHI + (unsigned)sw * 1024u);
                    LBAR();
                }
                else if (c == 9) WAIT_BAR(0);
                if (c < 8) {
                    const unsigned slot = lds0 + (unsigned)(c & 3) * K1_SLOT;
                    const int kc = (c + rot) & 7;
                    const char* xs = xb + kc * 128;
                    const unsigned xd = slot + (unsigned)sw * 4096u;
                    glds16(xs, xo0, xd);
                    glds16(xs + 8 * DM * 2, xo1, xd + 1024u);
                    glds16(xs + 16 * DM * 2, xo0, xd + 2048u);
                    glds16(xs + 24 * DM * 2, xo1, xd + 3072u);
                    const char* wsrc = wbp + kc * (64 * 1536 * 2);
                    const unsigned wd = slot + K1_WOFF + (unsigned)sw * 2048u;
#pragma unroll
                    for (int i = 0; i < 6; ++i)
                        glds16((i < 2 ? wqp + kc * (64 * 1536 * 2) : wsrc) + (i & 1) * (8 * 1536 * 2) + (i >> 1) * 1024, wo0, wd + (unsigned)(i >> 1) * 8192u + (unsigned)(i & 1) * 1024u);
                }
            }
        }
        asm volatile("" :: "v"(code_touch));
        const int peer = hi ? r32 * 8 + qb : hq * 32 + r32;
        const unsigned roff = WS_R + (unsigned)peer * 4u;
        const unsigned myf = WS_F + (unsigned)id * 512u + (unsigned)lane * 4u;
        const unsigned eo = WS_F + (unsigned)peer * 512u + (hi ? 128u + (unsigned)head * 4u : (unsigned)(id & 31) * 4u);
        unsigned rv = ld32(rs, roff);
        if (sw & 1) {
            const int gtok = qb * 256 + head * 8 + (sw >> 1) * 4 + (lane >> 4);
            float acc = 0.f;
#pragma unroll
            for (int i = 0; i < 4; ++i) {
                const bf16x8 gx = *(const bf16x8*)(x + (size_t)gtok * DM + i * 128 + (lane & 15) * 8), gv = *(const bf16x8*)(gw + i * 128 + (lane & 15) * 8);
#pragma unroll
                for (int j = 0; j < 8; ++j) acc = __builtin_fmaf(bf2f((u16)gx[j]), bf2f((u16)gv[j]), acc);
            }
#pragma unroll
            for (int m = 8; m >= 1; m >>= 1) acc += __shfl_xor(acc, m);
            if ((lane & 15) == 0) st32(rs, WS_G + (unsigned)gtok * 4u, __float_as_uint(1.f / (1.f + __expf(-acc))));
        }
        const unsigned nonce = __builtin_amdgcn_readlane(rv, id & 31);
        bool alive = true;
        auto wait_flags = [&](unsigned foff, unsigned long long mask, int resend) {
#pragma nounroll
            for (int spin = 0; alive; ++spin) {
                const unsigned v = ld32(rs, foff);
                if ((__ballot(v != nonce) & mask) == 0ull) break;
                if (resend && (spin & 3) == 3) {
                    const unsigned r2 = ld32(rs, roff);
                    if (hi == resend - 1 && r2 != rv) { st32(rs, eo, r2); rv = r2; }
                }
                if (spin >= SPIN_CAP) alive = false;
                __builtin_amdgcn_s_sleep(1);
            }
        };
        if (sw == 0) {
            lds_await(lds + L_E1, 8u);
            if (hi == 0) st32(rs, eo, rv);
            wait_flags(myf, (0xFull << ((xcd & 1) * 16 + (qb & 1) * 4)) | (0x3ull << ((qb >> 2) * 16 + 8 + ((2 * qb) & 7))), 1);
            *(volatile LAS unsigned*)(lds + L_E1 + 4) = 1u;
        } else lds_await(lds + L_E1 + 4, 1u);
        const char* kvb = ws + WS_KV + (size_t)(head * 64 + sw) * 2048;
        auto kv_block = [&](int r) {
            glds16x2_dev(kvb + ((r + blk0) & 15) * 8192, l16, lds0 + (unsigned)((r & 1) * 65536 + ((r >> 1) * 4 + sw) * 2048));
        };
        glds16x2_dev(ws + WS_Q + (size_t)(head * 64 + qb * 8 + sw * 2) * 1024, l16, lds0 + L_Q + (unsigned)sw * 2048u);
#pragma unroll 1
        for (int r = 0; r < 4; ++r) kv_block(r);
        const unsigned f1v = ld32(rs, myf);
        const bool f1full = (unsigned)__ballot(f1v != nonce) == 0u;
        WAIT_BAR(0);
        if (!f1full) wait_flags(myf, 0xFFFFFFFFull, sw == 0 ? 1 : 0);
#pragma unroll 1
        for (int r = 4; r < 8; ++r) kv_block(r);
        DRAIN();
        *(volatile LAS unsigned*)(lds + L_LAND + sw * 4) = 1u;
        if (br == 1) {
            const int g16 = lane >> 4, q4 = (lane & 15) >> 2, p4 = lane & 3;
            const int tu = ((g16 & 1) * 32 + p4 * 8) ^ ((2 * hi + (q4 >> 1)) * 16);
            const lds_ptr tp = lds + L_TIMG + ((sw >> 1) * 8 + (sw & 1)) * 2048 + (4 * hi + q4) * 64 + tu;
            const lds_ptr tq = lds + L_TIMG + ((sw >> 1) * 8 + (sw & 1)) * 2048 + (4 * hi + q4) * 64 + (tu ^ 8) + 512;
            const lds_ptr vr = lds + L_VA + (sw >> 1) * 8192 + (hi * 32 + (sw & 1) * 16 + (r32 & 15)) * 16;
            f32x16 kv = {};
#pragma unroll
            for (int i = 0; i < 8; ++i) {
                const bf16x8 a = cat8(lds_tr(tp + (i >> 1) * 4096 + (i & 1) * 1024), lds_tr(tq + (i >> 1) * 4096 + (i & 1) * 1024));
                const bf16x8 bv = lds_rd128(r32 < 16 ? vr + i * 1024 : lds + L_ONES);
                kv = MFMA(a, bv, kv);
            }
            if (r32 <= 16) {
                const unsigned o = WS_KVP + (unsigned)(((4 * hq + sw) * 16 + tb) * 4096 + (hi * 17 + r32) * 16);
#pragma unroll
                for (int g = 0; g < 4; ++g) st128(rs, o + g * 544u, __builtin_bit_cast(u32x4, (f32x4){kv[4 * g], kv[4 * g + 1], kv[4 * g + 2], kv[4 * g + 3]}));
            }
        } else {
            u32x4 pv[8];
#pragma unroll
            for (int i = 0; i < 8; ++i) pv[i] = *(const LAS u32x4*)(lds + L_TIMG + (((sw >> 1) * 4 + (i >> 1)) * 4 + (sw & 1) * 2 + (i & 1)) * 1024 + l16);
#pragma unroll
            for (int i = 0; i < 8; ++i) st128(rs, WS_PHIQ + (unsigned)(((4 * hq + sw) * 64 + tb * 4) * 2 + i) * 1024u + l16, pv[i]);
        }
        if (sw == 0) rv = ld32(rs, roff);
        lds_arrive(lds0 + L_KVS);
        lds_await(lds + L_KVS, 4u);
        if (sw == 0 && hi == 0) { st32(rs, eo, rv); st32(rs, eo + 384u, rv); }
#pragma unroll 1
        for (int r = 8; r < 16; ++r) kv_block(r);
        {
            const int row = (qb & 3) * 128 + (st & 127);
            const unsigned t = *(const unsigned*)((qb < 4 ? woa : wob) + row * DM + nt * 64);
            asm volatile("" :: "v"(t));
        }
        DRAIN();
        *(volatile LAS unsigned*)(lds + L_LAND + sw * 4) = 2u;
        wait_flags(myf + 256u, 0xFFFFFFFF00000000ull, 0);
        {
            u32x4 phq[4];
#pragma unroll
            for (int i = 0; i < 4; ++i) phq[i] = ld128(rs, WS_PHIQ + (unsigned)((head * 64 + qb * 8 + sw * 2) * 2 + i) * 1024u + l16);
            if (st < 136) {
                f32x4 acc = {};
#pragma unroll
                for (int p = 0; p < 16; ++p) acc += __builtin_bit_cast(f32x4, __builtin_amdgcn_raw_buffer_load_b128(rs, (unsigned)st * 16u, WS_KVP + (unsigned)(head * 16 + p) * 4096u, 16));
                const int gh = st / 17, c = st - gh * 17, g = gh >> 1;
                const lds_ptr dst = lds + L_KVF + (g >> 1) * 1024 + ((gh & 1) * 32 + c) * 16 + (g & 1) * 8;
                const u32x2 v = {cvtpk(acc[0], acc[1]), cvtpk(acc[2], acc[3])};
                *(LAS u32x2*)dst = v;
                if (c == 16) *(LAS u32x2*)(dst + 64) = v;
            }
            lds_arrive(lds0 + L_CNT);
            lds_await(lds + L_CNT, 4u);
            const bf16x8 kv0 = lds_rd128(lds + L_KVF + l16), kv1 = lds_rd128(lds + L_KVF + 1024 + l16);
#pragma unroll
            for (int i = 0; i < 2; ++i) {
                f32x16 a = MFMA(kv0, BF8(phq[2 * i]), zero);
                a = MFMA(kv1, BF8(phq[2 * i + 1]), a);
                st128(rs, WS_O + (unsigned)((qb * 8 + sw * 2 + i) * 64 + head) * 1024u + l16, pack8(a, 0, __builtin_amdgcn_rcpf(a[8] + 1e-6f)));
                st128(rs, WS_O + (unsigned)((qb * 8 + sw * 2 + i) * 64 + 32 + head) * 1024u + l16, (u32x4){EMPTY, EMPTY, EMPTY, EMPTY});
            }
            lds_arrive(lds0 + L_CNT + 4);
        }
        if (sw == 0) {
            lds_await(lds + L_CNT + 4, 4u);
            rv = ld32(rs, roff);
            if (hi) st32(rs, eo, rv);
            wait_flags(myf, 0xFFFFFFFF00000000ull, 2);
            rv = ld32(rs, roff);
            if (hi) st32(rs, eo, rv);
            *(LAS u32x4*)(lds + L_GATE + l16) = ld128(rs, WS_G + (unsigned)qb * 1024u + l16);
            asm volatile("s_waitcnt lgkmcnt(0)" ::: "memory");
            *(volatile LAS unsigned*)(lds + L_CNT + 12) = 4u;
        }
        const unsigned wo_off = (unsigned)(l3 * DM * 2) + swz1;
        const unsigned wo_dst = lds0 + (unsigned)(sw >> 1) * 65536u + (unsigned)(sw & 1) * 16384u;
        const size_t wo_base = (size_t)(sw * 128 * DM + nt * 64) * 2;
        auto wait_prog = [&](unsigned v, int slp) {
#pragma nounroll
            for (int spin = 0; spin < 64 * SPIN_CAP; ++spin) {
                if (__ballot(lds_ld(lds + L_PROG + (lane & 7) * 4) <= v) == 0ull) break;
                if (slp) __builtin_amdgcn_s_sleep(8); else __builtin_amdgcn_s_sleep(1);
            }
            asm volatile("" ::: "memory");
        };
#pragma unroll 1
        for (int stage = 2; stage <= 3; ++stage) {
            wait_prog((unsigned)stage, 1);
            const char* src = (const char*)(stage == 2 ? woa : wob) + wo_base;
            const unsigned dst = wo_dst + (stage == 2 ? 0u : 32768u);
            const int i1 = (stage == 3 && (sw & 1)) ? 8 : 16;
#pragma unroll 1
            for (int i = 0; i < i1; ++i) glds16(src + i * (8 * DM * 2), wo_off, dst + (unsigned)i * 1024u);
            if (stage == 2) lds_arrive(lds0 + L_KVD);
        }
        if (sw & 1) {
            u32x4 wl[8];
#pragma unroll
            for (int i = 0; i < 8; ++i) wl[i] = *(const u32x4*)((const char*)wob + wo_base + (8 + i) * (8 * DM * 2) + wo_off);
#pragma unroll
            for (int i = 0; i < 8; ++i) asm volatile("" : "+v"(wl[i]));
            wait_prog(4u, 0);
#pragma unroll
            for (int i = 0; i < 8; ++i) *(LAS u32x4*)(uintptr_t)(wo_dst + 32768u + (unsigned)(8 + i) * 1024u + l16) = wl[i];
        }
        lds_arrive(lds0 + L_WOB);
        if (sw == 0) {
            wait_prog(4u, 0);
            if (hi) st32(rs, eo + 128u, rv);
        }
        if (sw == 2) {
#pragma nounroll
            for (int spin = 0;; ++spin) {
                const unsigned v = ld32(rs, myf + 256u);
                unsigned m = (unsigned)__ballot(v == nonce);
                if (spin >= SPIN_CAP || !alive) m = 0xFFFFFFFFu;
                *(volatile LAS unsigned*)(lds + L_MASK) = m;
                if (m == 0xFFFFFFFFu) break;
            }
        }
    }

}

extern "C" void kernel_launch(void* const* d_in, const int* in_sizes, int n_in, void* d_out, int out_size, void* d_ws, size_t ws_size,
                              hipStream_t stream) {
    (void)hipFuncSetAttribute((const void*)fused_kernel, hipFuncAttributeMaxDynamicSharedMemorySize, LDS_BYTES);
    fused_kernel<<<256, 768, LDS_BYTES, stream>>>((const u16*)d_in[0], (const u16*)d_in[1], (const u16*)d_in[5], (const u16*)d_in[2], (const u16*)d_in[7],
                                                  (const u16*)d_in[3], (const u16*)d_in[6], (char*)d_ws, (u16*)d_out);
}
```
